```python
import torch

def _softmax(x, axis=-1):
    return torch.softmax(x, dim=axis)
def _gelu(x):
    return torch.nn.functional.gelu(x, approximate="tanh")
def _silu(x):
    return torch.nn.functional.silu(x)
def _sigmoid(x):
    return torch.sigmoid(x)
def _layernorm(x, axis=-1, eps=1e-5):
    m = x.mean(dim=axis, keepdim=True)
    v = x.var(dim=axis, keepdim=True, unbiased=False)
    return (x - m) / torch.sqrt(v + eps)
def _scatter_add(tgt, idx, src, axis=0):
    return tgt.clone().index_add_(axis, idx, src)
def _segment_sum(src, seg, n):
    out = torch.zeros((n,) + src.shape[1:], dtype=src.dtype)
    return out.index_add_(0, seg, src)
def _pad(x, pw):
    flat = tuple(v for pair in reversed(pw) for v in pair)
    return torch.nn.functional.pad(x, flat)

kernel_name = 'gated_mix__softmax_full_mha__dense_activation_relu__prompt'

def setup_inputs():

    _w = (lambda a: (lambda t: t.to(torch.bfloat16) if t.is_floating_point() else t)(torch.as_tensor(a)))
    def _A_setup():
        B, S, D = 1, 2048, 512
        return {
            "x": _w((lambda *s: torch.randn(*s, dtype=torch.bfloat16) * 0.05)(B, S, D)),
            "wqkv": _w((lambda *s: torch.randn(*s, dtype=torch.bfloat16) * 0.8)(D, 3 * D)),
            "wo": _w((lambda *s: torch.randn(*s, dtype=torch.bfloat16) * 0.1)(D, D)),
        }
    _A_in = _A_setup()

    def _B_setup():
        B, S, D, Dff = 1, 2048, 512, 1376
        return {
            "x": _w((lambda *s: torch.randn(*s, dtype=torch.bfloat16) * 0.05)(B, S, D)),
            "w1": _w((lambda *s: torch.randn(*s, dtype=torch.bfloat16) * 0.05)(D, Dff)),
            "w2": _w((lambda *s: torch.randn(*s, dtype=torch.bfloat16) * 0.05)(Dff, D)),
        }
    _B_in = _B_setup()

    return {
        "A_x": _A_in["x"],
        "A_wqkv": _A_in["wqkv"],
        "A_wo": _A_in["wo"],
        "B_x": _B_in["x"],
        "B_w1": _B_in["w1"],
        "B_w2": _B_in["w2"],
        "gate_w": _w((lambda *s: torch.randn(*s, dtype=torch.bfloat16) * 0.05)(512) * 16.0),
    }

def reference(A_x, A_wqkv, A_wo, B_x, B_w1, B_w2, gate_w):
    def _A_ref(x, wqkv, wo):
        B, S, D = x.shape
        H = 32; Dh = D // H
        qkv = (lambda x, *d: x.permute(*d))((x @ wqkv).reshape(B, S, 3, H, Dh), 2, 0, 3, 1, 4)
        q, k, v = qkv[0], qkv[1], qkv[2]
        scale = Dh ** -0.5
        scores = torch.einsum("bhqd,bhkd->bhqk", q, k) * scale
        p = _softmax(scores, axis=-1)
        o = torch.einsum("bhqk,bhkd->bhqd", p, v)
        return (lambda x, *d: x.permute(*d))(o, 0, 2, 1, 3).reshape(B, S, D) @ wo

    def _B_ref(x, w1, w2):
        h = torch.einsum("bsd,df->bsf", x, w1)
        zero = torch.zeros_like(h)
        h = torch.maximum(h, zero)
        return torch.einsum("bsf,fd->bsd", h, w2)

    x = A_x
    g = _sigmoid((x * gate_w).sum(-1, keepdims=True))
    a = _A_ref(x, A_wqkv, A_wo)
    b = _B_ref(x, B_w1, B_w2)
    return g * a + (1 - g) * b
```

```cpp
#include <hip/hip_runtime.h>
#include <hip/hip_bf16.h>
#include <stdint.h>

#define SEQ 2048
#define DM 512
#define NH 32
#define DH 16
#define DFF 1376
#define NQKV 1536

typedef __attribute__((ext_vector_type(8))) short bf16x8;
typedef __attribute__((ext_vector_type(4))) short bf16x4;
typedef __attribute__((ext_vector_type(16))) float f32x16;
typedef __attribute__((ext_vector_type(4))) float f32x4;
typedef __attribute__((ext_vector_type(4))) unsigned u32x4;
typedef __attribute__((ext_vector_type(2))) unsigned u32x2;
typedef unsigned short u16;

#define WS_Q   (0u)
#define WS_K   (2u << 20)
#define WS_V   (4u << 20)
#define WS_H   (6u << 20)
#define WS_O   (12u << 20)
#define WS_G   (14u << 20)

__device__ __forceinline__ unsigned pack2(float a, float b) {
  __hip_bfloat162 t = __float22bfloat162_rn(make_float2(a, b));
  return *reinterpret_cast<unsigned*>(&t);
}
__device__ __forceinline__ float bf2f(u16 v) { return __uint_as_float(((unsigned)v) << 16); }

__device__ __forceinline__ bf16x4 tr_read(const u16* p) {
  typedef __attribute__((ext_vector_type(4))) short v4s;
  return __builtin_amdgcn_ds_read_tr16_b64_v4i16((__attribute__((address_space(3))) v4s*)(p));
}

__device__ __forceinline__ f32x16 mfma32(bf16x8 a, bf16x8 b, f32x16 c) {
  return __builtin_amdgcn_mfma_f32_32x32x16_bf16(a, b, c, 0, 0, 0);
}

#define K1_BM 128
#define K1_BN 128
#define K1_BK 64
#define K1_LDA 72
#define K1_LDB 160
#define K1_NT 23
#define K1_MT 16

__global__ __launch_bounds__(256) void k1_proj(const u16* __restrict__ x, const u16* __restrict__ wqkv,
                                               const u16* __restrict__ w1, const u16* __restrict__ gate_w,
                                               unsigned char* __restrict__ ws) {
  __shared__ __attribute__((aligned(16))) u16 sA[2][K1_BM][K1_LDA];
  __shared__ __attribute__((aligned(16))) u16 sB[2][K1_BK][K1_LDB];
  const int tid = threadIdx.x;
  const int lane = tid & 63;
  const int wave = tid >> 6;
  const int bid = blockIdx.x;

  if (bid >= K1_NT * K1_MT) {
    const int r0 = (bid - K1_NT * K1_MT) * 128 + wave * 32;
    float* g = reinterpret_cast<float*>(ws + WS_G);
    bf16x8 gw = *reinterpret_cast<const bf16x8*>(gate_w + lane * 8);
    for (int r = r0; r < r0 + 32; ++r) {
      bf16x8 xv = *reinterpret_cast<const bf16x8*>(x + (size_t)r * DM + lane * 8);
      float s = 0.f;
#pragma unroll
      for (int j = 0; j < 8; ++j) s += bf2f((u16)xv[j]) * bf2f((u16)gw[j]);
#pragma unroll
      for (int o = 32; o > 0; o >>= 1) s += __shfl_xor(s, o);
      if (lane == 0) g[r] = 1.f / (1.f + __expf(-s));
    }
    return;
  }

  const int nt = bid % K1_NT;
  const int mt = bid / K1_NT;
  const int m0 = mt * K1_BM;
  const int n0 = nt * K1_BN;
  const u16* W;
  int ldw, ncol0, nlim;
  if (nt < 12) { W = wqkv; ldw = NQKV; ncol0 = n0; nlim = NQKV; }
  else { W = w1; ldw = DFF; ncol0 = n0 - NQKV; nlim = DFF; }
  const bool isV = (nt >= 8 && nt < 12);

  const int wm = wave >> 1, wn = wave & 1;
  const int h = lane >> 5, l31 = lane & 31;

  f32x16 acc[2][2];
#pragma unroll
  for (int i = 0; i < 2; ++i)
#pragma unroll
    for (int j = 0; j < 2; ++j)
#pragma unroll
      for (int r = 0; r < 16; ++r) acc[i][j][r] = 0.f;

  u32x4 ra[4], rb[4];
  const int a_row = tid >> 3, a_col = (tid & 7) * 8;
  const int b_row = tid >> 4, b_col = (tid & 15) * 8;
  const bool b_ok = (ncol0 + b_col + 8) <= nlim;

  auto gload = [&](int kt) {
#pragma unroll
    for (int i = 0; i < 4; ++i)
      ra[i] = *reinterpret_cast<const u32x4*>(x + (size_t)(m0 + a_row + 32 * i) * DM + kt * K1_BK + a_col);
#pragma unroll
    for (int i = 0; i < 4; ++i) {
      if (b_ok) rb[i] = *reinterpret_cast<const u32x4*>(W + (size_t)(kt * K1_BK + b_row + 16 * i) * ldw + ncol0 + b_col);
      else rb[i] = (u32x4){0u, 0u, 0u, 0u};
    }
  };
  auto swrite = [&](int buf) {
#pragma unroll
    for (int i = 0; i < 4; ++i) *reinterpret_cast<u32x4*>(&sA[buf][a_row + 32 * i][a_col]) = ra[i];
#pragma unroll
    for (int i = 0; i < 4; ++i) *reinterpret_cast<u32x4*>(&sB[buf][b_row + 16 * i][b_col]) = rb[i];
  };

  gload(0);
  swrite(0);
  __syncthreads();
  const int NKT = DM / K1_BK;
  const int blk16 = (lane >> 4) & 1, q4 = (lane & 15) >> 2, p4 = lane & 3;
  for (int kt = 0; kt < NKT; ++kt) {
    const int buf = kt & 1;
    if (kt + 1 < NKT) gload(kt + 1);
#pragma unroll
    for (int s = 0; s < 4; ++s) {
      bf16x8 af[2], bfr[2];
#pragma unroll
      for (int mi = 0; mi < 2; ++mi)
        af[mi] = *reinterpret_cast<const bf16x8*>(&sA[buf][wm * 64 + mi * 32 + l31][s * 16 + 8 * h]);
#pragma unroll
      for (int ni = 0; ni < 2; ++ni) {
        bf16x4 t0 = tr_read(&sB[buf][s * 16 + 8 * h + q4][wn * 64 + ni * 32 + 16 * blk16 + 4 * p4]);
        bf16x4 t1 = tr_read(&sB[buf][s * 16 + 8 * h + 4 + q4][wn * 64 + ni * 32 + 16 * blk16 + 4 * p4]);
        bfr[ni] = (bf16x8){t0[0], t0[1], t0[2], t0[3], t1[0], t1[1], t1[2], t1[3]};
      }
      if (isV) {
#pragma unroll
        for (int ni = 0; ni < 2; ++ni)
#pragma unroll
          for (int mi = 0; mi < 2; ++mi) acc[ni][mi] = mfma32(af[mi], bfr[ni], acc[ni][mi]);
      } else {
#pragma unroll
        for (int ni = 0; ni < 2; ++ni)
#pragma unroll
          for (int mi = 0; mi < 2; ++mi) acc[ni][mi] = mfma32(bfr[ni], af[mi], acc[ni][mi]);
      }
    }
    if (kt + 1 < NKT) {
      swrite(buf ^ 1);
      __syncthreads();
    }
  }

  if (isV) {
    u16* vt = reinterpret_cast<u16*>(ws + WS_V);
#pragma unroll
    for (int ni = 0; ni < 2; ++ni) {
      const int nv = n0 - 1024 + wn * 64 + ni * 32 + l31;
      const int head = nv >> 4, d = nv & 15;
#pragma unroll
      for (int mi = 0; mi < 2; ++mi) {
#pragma unroll
        for (int g = 0; g < 4; ++g) {
          const int key = m0 + wm * 64 + mi * 32 + 8 * g + 4 * h;
          const int kb = key >> 4, kq = key & 15;
          const int jhi = kq >> 3, hh = (kq >> 2) & 1;
          u32x2 v;
          v[0] = pack2(acc[ni][mi][4 * g], acc[ni][mi][4 * g + 1]);
          v[1] = pack2(acc[ni][mi][4 * g + 2], acc[ni][mi][4 * g + 3]);
          *reinterpret_cast<u32x2*>(vt + (((size_t)head * 128 + kb) * 16 + d) * 16 + hh * 8 + jhi * 4) = v;
        }
      }
    }
  } else {
    const float qscale = 0.25f * 1.4426950408889634f;
#pragma unroll
    for (int ni = 0; ni < 2; ++ni) {
#pragma unroll
      for (int mi = 0; mi < 2; ++mi) {
        const int tok = m0 + wm * 64 + mi * 32 + l31;
#pragma unroll
        for (int g = 0; g < 4; ++g) {
          const int n = n0 + wn * 64 + ni * 32 + 8 * g + 4 * h;
          float v0 = acc[ni][mi][4 * g], v1 = acc[ni][mi][4 * g + 1], v2 = acc[ni][mi][4 * g + 2], v3 = acc[ni][mi][4 * g + 3];
          if (n < 512) {
            u32x2 v;
            v[0] = pack2(v0 * qscale, v1 * qscale);
            v[1] = pack2(v2 * qscale, v3 * qscale);
            *reinterpret_cast<u32x2*>(reinterpret_cast<u16*>(ws + WS_Q) + ((size_t)(n >> 4) * SEQ + tok) * 16 + (n & 15)) = v;
          } else if (n < 1024) {
            const int nk = n - 512;
            u32x2 v;
            v[0] = pack2(v0, v1);
            v[1] = pack2(v2, v3);
            *reinterpret_cast<u32x2*>(reinterpret_cast<u16*>(ws + WS_K) + ((size_t)(nk >> 4) * SEQ + tok) * 16 + (nk & 15)) = v;
          } else {
            const int nf = n - NQKV;
            if (nf < DFF) {
              u32x2 v;
              v[0] = pack2(fmaxf(v0, 0.f), fmaxf(v1, 0.f));
              v[1] = pack2(fmaxf(v2, 0.f), fmaxf(v3, 0.f));
              *reinterpret_cast<u32x2*>(reinterpret_cast<u16*>(ws + WS_H) + (size_t)tok * DFF + nf) = v;
            }
          }
        }
      }
    }
  }
}

#define AT_KC 128

__global__ __launch_bounds__(256) void k2_attn(unsigned char* __restrict__ ws) {
  __shared__ __attribute__((aligned(16))) u16 sK[2][AT_KC * 16];
  __shared__ __attribute__((aligned(16))) u16 sV[2][AT_KC * 16];
  const int tid = threadIdx.x, lane = tid & 63, wave = tid >> 6;
  const int head = blockIdx.x >> 4;
  const int qt = blockIdx.x & 15;
  const int h = lane >> 5, l31 = lane & 31;
  const u16* qg = reinterpret_cast<const u16*>(ws + WS_Q) + (size_t)head * SEQ * 16;
  const u16* kg = reinterpret_cast<const u16*>(ws + WS_K) + (size_t)head * SEQ * 16;
  const u16* vg = reinterpret_cast<const u16*>(ws + WS_V) + (size_t)head * SEQ * 16;

  const int q0 = qt * 128 + wave * 32;
  const bf16x8 qf = *reinterpret_cast<const bf16x8*>(qg + (size_t)(q0 + l31) * 16 + 8 * h);

  bf16x8 vconst;
  {
    short one = (l31 == 16) ? (short)0x3F80 : (short)0;
#pragma unroll
    for (int j = 0; j < 8; ++j) vconst[j] = one;
  }
  const bool vlow = l31 < 16;

  f32x16 oacc;
#pragma unroll
  for (int r = 0; r < 16; ++r) oacc[r] = 0.f;
  float m = -INFINITY;

  u32x4 rk, rv;
  rk = *reinterpret_cast<const u32x4*>(kg + tid * 8);
  rv = *reinterpret_cast<const u32x4*>(vg + tid * 8);
  *reinterpret_cast<u32x4*>(&sK[0][tid * 8]) = rk;
  *reinterpret_cast<u32x4*>(&sV[0][tid * 8]) = rv;
  __syncthreads();

  const int NC = SEQ / AT_KC;
  for (int c = 0; c < NC; ++c) {
    const int buf = c & 1;
    if (c + 1 < NC) {
      rk = *reinterpret_cast<const u32x4*>(kg + (size_t)(c + 1) * AT_KC * 16 + tid * 8);
      rv = *reinterpret_cast<const u32x4*>(vg + (size_t)(c + 1) * AT_KC * 16 + tid * 8);
    }
#pragma unroll
    for (int t = 0; t < AT_KC / 64; ++t) {
      bf16x8 kf0 = *reinterpret_cast<const bf16x8*>(&sK[buf][(t * 64 + l31) * 16 + 8 * h]);
      bf16x8 kf1 = *reinterpret_cast<const bf16x8*>(&sK[buf][(t * 64 + 32 + l31) * 16 + 8 * h]);
      f32x16 z;
#pragma unroll
      for (int r = 0; r < 16; ++r) z[r] = 0.f;
      f32x16 s0 = mfma32(kf0, qf, z);
      f32x16 s1 = mfma32(kf1, qf, z);
      float tm = s0[0];
#pragma unroll
      for (int r = 1; r < 16; ++r) tm = fmaxf(tm, s0[r]);
#pragma unroll
      for (int r = 0; r < 16; ++r) tm = fmaxf(tm, s1[r]);
      {
        auto rr = __builtin_amdgcn_permlane32_swap(__float_as_uint(tm), __float_as_uint(tm), false, false);
        tm = fmaxf(__uint_as_float(rr[0]), __uint_as_float(rr[1]));
      }
      const float mn = fmaxf(m, tm);
      const float alpha = exp2f(m - mn);
      m = mn;
#pragma unroll
      for (int r = 0; r < 9; ++r) oacc[r] *= alpha;
      bf16x8 p[4];
#pragma unroll
      for (int ss = 0; ss < 2; ++ss) {
        u32x4 w0, w1;
#pragma unroll
        for (int jj = 0; jj < 4; ++jj) {
          w0[jj] = pack2(exp2f(s0[8 * ss + 2 * jj] - mn), exp2f(s0[8 * ss + 2 * jj + 1] - mn));
          w1[jj] = pack2(exp2f(s1[8 * ss + 2 * jj] - mn), exp2f(s1[8 * ss + 2 * jj + 1] - mn));
        }
        p[ss] = __builtin_bit_cast(bf16x8, w0);
        p[2 + ss] = __builtin_bit_cast(bf16x8, w1);
      }
#pragma unroll
      for (int kb = 0; kb < 4; ++kb) {
        bf16x8 vf = vconst;
        if (vlow) vf = *reinterpret_cast<const bf16x8*>(&sV[buf][((t * 4 + kb) * 16 + l31) * 16 + 8 * h]);
        oacc = mfma32(vf, p[kb], oacc);
      }
    }
    if (c + 1 < NC) {
      *reinterpret_cast<u32x4*>(&sK[buf ^ 1][tid * 8]) = rk;
      *reinterpret_cast<u32x4*>(&sV[buf ^ 1][tid * 8]) = rv;
      __syncthreads();
    }
  }
  float l;
  {
    auto rr = __builtin_amdgcn_permlane32_swap(__float_as_uint(oacc[8]), __float_as_uint(oacc[8]), false, false);
    l = __uint_as_float(rr[0]);
  }
  const float inv = 1.f / l;
  u16* og = reinterpret_cast<u16*>(ws + WS_O) + (size_t)(q0 + l31) * DM + head * 16;
#pragma unroll
  for (int g = 0; g < 2; ++g) {
    u32x2 v;
    v[0] = pack2(oacc[4 * g] * inv, oacc[4 * g + 1] * inv);
    v[1] = pack2(oacc[4 * g + 2] * inv, oacc[4 * g + 3] * inv);
    *reinterpret_cast<u32x2*>(og + 8 * g + 4 * h) = v;
  }
}

#define K3_BM 64
#define K3_BN 64
#define K3_BK 32
#define K3_LDA 40
#define K3_LDB 96

__global__ __launch_bounds__(256) void k3_out(const unsigned char* __restrict__ ws, const u16* __restrict__ wo,
                                              const u16* __restrict__ w2, u16* __restrict__ out) {
  __shared__ __attribute__((aligned(16))) u16 sA[2][K3_BM][K3_LDA];
  __shared__ __attribute__((aligned(16))) u16 sB[2][K3_BK][K3_LDB];
  const int tid = threadIdx.x, lane = tid & 63, wave = tid >> 6;
  const int nt = blockIdx.x & 7, mt = blockIdx.x >> 3;
  const int m0 = mt * K3_BM, n0 = nt * K3_BN;
  const int wm = wave >> 1, wn = wave & 1;
  const int h = lane >> 5, l31 = lane & 31;
  const u16* og = reinterpret_cast<const u16*>(ws + WS_O);
  const u16* hg = reinterpret_cast<const u16*>(ws + WS_H);

  f32x16 acc1, acc2;
#pragma unroll
  for (int r = 0; r < 16; ++r) { acc1[r] = 0.f; acc2[r] = 0.f; }

  const int a_row = tid >> 2, a_col = (tid & 3) * 8;
  const int b_row = tid >> 3, b_col = (tid & 7) * 8;
  u32x4 ra, rb;
  const int NK1 = DM / K3_BK;
  const int NKT = NK1 + DFF / K3_BK;
  auto gload = [&](int kt) {
    if (kt < NK1) {
      ra = *reinterpret_cast<const u32x4*>(og + (size_t)(m0 + a_row) * DM + kt * K3_BK + a_col);
      rb = *reinterpret_cast<const u32x4*>(wo + (size_t)(kt * K3_BK + b_row) * DM + n0 + b_col);
    } else {
      const int k2 = kt - NK1;
      ra = *reinterpret_cast<const u32x4*>(hg + (size_t)(m0 + a_row) * DFF + k2 * K3_BK + a_col);
      rb = *reinterpret_cast<const u32x4*>(w2 + (size_t)(k2 * K3_BK + b_row) * DM + n0 + b_col);
    }
  };
  auto swrite = [&](int buf) {
    *reinterpret_cast<u32x4*>(&sA[buf][a_row][a_col]) = ra;
    *reinterpret_cast<u32x4*>(&sB[buf][b_row][b_col]) = rb;
  };
  gload(0);
  swrite(0);
  __syncthreads();
  const int blk16 = (lane >> 4) & 1, q4 = (lane & 15) >> 2, p4 = lane & 3;
  for (int kt = 0; kt < NKT; ++kt) {
    const int buf = kt & 1;
    if (kt + 1 < NKT) gload(kt + 1);
#pragma unroll
    for (int s = 0; s < 2; ++s) {
      bf16x8 af = *reinterpret_cast<const bf16x8*>(&sA[buf][wm * 32 + l31][s * 16 + 8 * h]);
      bf16x4 t0 = tr_read(&sB[buf][s * 16 + 8 * h + q4][wn * 32 + 16 * blk16 + 4 * p4]);
      bf16x4 t1 = tr_read(&sB[buf][s * 16 + 8 * h + 4 + q4][wn * 32 + 16 * blk16 + 4 * p4]);
      bf16x8 bfr = (bf16x8){t0[0], t0[1], t0[2], t0[3], t1[0], t1[1], t1[2], t1[3]};
      if (kt < NK1) acc1 = mfma32(bfr, af, acc1);
      else acc2 = mfma32(bfr, af, acc2);
    }
    if (kt + 1 < NKT) {
      swrite(buf ^ 1);
      __syncthreads();
    }
  }
  const int tok = m0 + wm * 32 + l31;
  const float g = reinterpret_cast<const float*>(ws + WS_G)[tok];
  const float g1 = 1.f - g;
#pragma unroll
  for (int gq = 0; gq < 4; ++gq) {
    const int n = n0 + wn * 32 + 8 * gq + 4 * h;
    u32x2 v;
    v[0] = pack2(g * acc1[4 * gq] + g1 * acc2[4 * gq], g * acc1[4 * gq + 1] + g1 * acc2[4 * gq + 1]);
    v[1] = pack2(g * acc1[4 * gq + 2] + g1 * acc2[4 * gq + 2], g * acc1[4 * gq + 3] + g1 * acc2[4 * gq + 3]);
    *reinterpret_cast<u32x2*>(out + (size_t)tok * DM + n) = v;
  }
}

extern "C" void kernel_launch(void* const* d_in, const int* in_sizes, int n_in,
                              void* d_out, int out_size, void* d_ws, size_t ws_size,
                              hipStream_t stream) {
  const u16* x = (const u16*)d_in[0];
  const u16* wqkv = (const u16*)d_in[1];
  const u16* wo = (const u16*)d_in[2];
  const u16* w1 = (const u16*)d_in[4];
  const u16* w2 = (const u16*)d_in[5];
  const u16* gw = (const u16*)d_in[6];
  unsigned char* ws = (unsigned char*)d_ws;
  k1_proj<<<K1_NT * K1_MT + 16, 256, 0, stream>>>(x, wqkv, w1, gw, ws);
  k2_attn<<<NH * 16, 256, 0, stream>>>(ws);
  k3_out<<<(SEQ / K3_BM) * (DM / K3_BN), 256, 0, stream>>>(ws, wo, w2, (u16*)d_out);
}
```

```cpp
#include <hip/hip_runtime.h>
#include <hip/hip_bf16.h>
#include <stdint.h>

#define SEQ 2048
#define DM 512
#define NH 32
#define DH 16
#define DFF 1376
#define NQKV 1536

typedef __attribute__((ext_vector_type(8))) short bf16x8;
typedef __attribute__((ext_vector_type(4))) short bf16x4;
typedef __attribute__((ext_vector_type(16))) float f32x16;
typedef __attribute__((ext_vector_type(4))) float f32x4;
typedef __attribute__((ext_vector_type(4))) unsigned u32x4;
typedef __attribute__((ext_vector_type(2))) unsigned u32x2;
typedef unsigned short u16;

#define WS_Q   (0u)
#define WS_K   (2u << 20)
#define WS_V   (4u << 20)
#define WS_H   (6u << 20)
#define WS_O   (12u << 20)
#define WS_G   (14u << 20)
#define WS_B   (16u << 20)

__device__ __forceinline__ unsigned pack2(float a, float b) {
  typedef __attribute__((ext_vector_type(2))) float f2;
  typedef __attribute__((ext_vector_type(2))) __bf16 b2;
  b2 t = __builtin_convertvector((f2){a, b}, b2);
  return __builtin_bit_cast(unsigned, t);
}
__device__ __forceinline__ float bf2f(u16 v) { return __uint_as_float(((unsigned)v) << 16); }

__device__ __forceinline__ bf16x4 tr_read(const u16* p) {
  typedef __attribute__((ext_vector_type(4))) short v4s;
  return __builtin_amdgcn_ds_read_tr16_b64_v4i16((__attribute__((address_space(3))) v4s*)(p));
}

__device__ __forceinline__ f32x16 mfma32(bf16x8 a, bf16x8 b, f32x16 c) {
  return __builtin_amdgcn_mfma_f32_32x32x16_bf16(a, b, c, 0, 0, 0);
}

#define K1_BM 128
#define K1_BN 128
#define K1_BK 64
#define K1_LDA 72
#define K1_LDB 160
#define K1_NT 23
#define K1_MT 16

template <bool isV>
__device__ __forceinline__ void k1_body(const u16* __restrict__ x, const u16* __restrict__ wqkv,
                                        const u16* __restrict__ w1, unsigned char* __restrict__ ws,
                                        u16 (*sA)[K1_BM][K1_LDA], u16 (*sB)[K1_BK][K1_LDB], int nt, int mt) {
  const int tid = threadIdx.x;
  const int lane = tid & 63;
  const int wave = tid >> 6;

  const int m0 = mt * K1_BM;
  const int n0 = nt * K1_BN;
  const u16* W;
  int ldw, ncol0, nlim;
  if (nt < 12) { W = wqkv; ldw = NQKV; ncol0 = n0; nlim = NQKV; }
  else { W = w1; ldw = DFF; ncol0 = n0 - NQKV; nlim = DFF; }

  const int wm = wave >> 1, wn = wave & 1;
  const int h = lane >> 5, l31 = lane & 31;

  f32x16 acc[2][2];
#pragma unroll
  for (int i = 0; i < 2; ++i)
#pragma unroll
    for (int j = 0; j < 2; ++j)
#pragma unroll
      for (int r = 0; r < 16; ++r) acc[i][j][r] = 0.f;

  u32x4 ra[4], rb[4];
  const int a_row = tid >> 3, a_col = (tid & 7) * 8;
  const int b_row = tid >> 4, b_col = (tid & 15) * 8;
  const bool b_ok = (ncol0 + b_col + 8) <= nlim;

  auto gload = [&](int kt) {
#pragma unroll
    for (int i = 0; i < 4; ++i)
      ra[i] = *reinterpret_cast<const u32x4*>(x + (size_t)(m0 + a_row + 32 * i) * DM + kt * K1_BK + a_col);
#pragma unroll
    for (int i = 0; i < 4; ++i) {
      if (b_ok) rb[i] = *reinterpret_cast<const u32x4*>(W + (size_t)(kt * K1_BK + b_row + 16 * i) * ldw + ncol0 + b_col);
      else rb[i] = (u32x4){0u, 0u, 0u, 0u};
    }
  };
  auto swrite = [&](int buf) {
#pragma unroll
    for (int i = 0; i < 4; ++i) *reinterpret_cast<u32x4*>(&sA[buf][a_row + 32 * i][a_col]) = ra[i];
#pragma unroll
    for (int i = 0; i < 4; ++i) *reinterpret_cast<u32x4*>(&sB[buf][b_row + 16 * i][b_col]) = rb[i];
  };

  gload(0);
  swrite(0);
  __syncthreads();
  const int NKT = DM / K1_BK;
  const int blk16 = (lane >> 4) & 1, q4 = (lane & 15) >> 2, p4 = lane & 3;
  for (int kt = 0; kt < NKT; ++kt) {
    const int buf = kt & 1;
    if (kt + 1 < NKT) gload(kt + 1);
#pragma unroll
    for (int s = 0; s < 4; ++s) {
      bf16x8 af[2], bfr[2];
#pragma unroll
      for (int mi = 0; mi < 2; ++mi)
        af[mi] = *reinterpret_cast<const bf16x8*>(&sA[buf][wm * 64 + mi * 32 + l31][s * 16 + 8 * h]);
#pragma unroll
      for (int ni = 0; ni < 2; ++ni) {
        bf16x4 t0 = tr_read(&sB[buf][s * 16 + 8 * h + q4][wn * 64 + ni * 32 + 16 * blk16 + 4 * p4]);
        bf16x4 t1 = tr_read(&sB[buf][s * 16 + 8 * h + 4 + q4][wn * 64 + ni * 32 + 16 * blk16 + 4 * p4]);
        bfr[ni] = (bf16x8){t0[0], t0[1], t0[2], t0[3], t1[0], t1[1], t1[2], t1[3]};
      }
      if (isV) {
#pragma unroll
        for (int ni = 0; ni < 2; ++ni)
#pragma unroll
          for (int mi = 0; mi < 2; ++mi) acc[ni][mi] = mfma32(af[mi], bfr[ni], acc[ni][mi]);
      } else {
#pragma unroll
        for (int ni = 0; ni < 2; ++ni)
#pragma unroll
          for (int mi = 0; mi < 2; ++mi) acc[ni][mi] = mfma32(bfr[ni], af[mi], acc[ni][mi]);
      }
    }
    if (kt + 1 < NKT) {
      swrite(buf ^ 1);
      __syncthreads();
    }
  }

  if (isV) {
    u16* vt = reinterpret_cast<u16*>(ws + WS_V);
#pragma unroll
    for (int ni = 0; ni < 2; ++ni) {
      const int nv = n0 - 1024 + wn * 64 + ni * 32 + l31;
      const int head = nv >> 4, d = nv & 15;
#pragma unroll
      for (int mi = 0; mi < 2; ++mi) {
#pragma unroll
        for (int g = 0; g < 4; ++g) {
          const int key = m0 + wm * 64 + mi * 32 + 8 * g + 4 * h;
          const int kb = key >> 4, kq = key & 15;
          const int jhi = kq >> 3, hh = (kq >> 2) & 1;
          u32x2 v;
          v[0] = pack2(acc[ni][mi][4 * g], acc[ni][mi][4 * g + 1]);
          v[1] = pack2(acc[ni][mi][4 * g + 2], acc[ni][mi][4 * g + 3]);
          *reinterpret_cast<u32x2*>(vt + (((size_t)head * 128 + kb) * 16 + d) * 16 + hh * 8 + jhi * 4) = v;
        }
      }
    }
  } else {
    const float qscale = 0.25f * 1.4426950408889634f;
#pragma unroll
    for (int ni = 0; ni < 2; ++ni) {
#pragma unroll
      for (int mi = 0; mi < 2; ++mi) {
        const int tok = m0 + wm * 64 + mi * 32 + l31;
#pragma unroll
        for (int g = 0; g < 4; ++g) {
          const int n = n0 + wn * 64 + ni * 32 + 8 * g + 4 * h;
          float v0 = acc[ni][mi][4 * g], v1 = acc[ni][mi][4 * g + 1], v2 = acc[ni][mi][4 * g + 2], v3 = acc[ni][mi][4 * g + 3];
          if (n < 512) {
            u32x2 v;
            v[0] = pack2(v0 * qscale, v1 * qscale);
            v[1] = pack2(v2 * qscale, v3 * qscale);
            *reinterpret_cast<u32x2*>(reinterpret_cast<u16*>(ws + WS_Q) + ((size_t)(n >> 4) * SEQ + tok) * 16 + (n & 15)) = v;
          } else if (n < 1024) {
            const int nk = n - 512;
            u32x2 v;
            v[0] = pack2(v0, v1);
            v[1] = pack2(v2, v3);
            *reinterpret_cast<u32x2*>(reinterpret_cast<u16*>(ws + WS_K) + ((size_t)(nk >> 4) * SEQ + tok) * 16 + (nk & 15)) = v;
          } else {
            const int nf = n - NQKV;
            if (nf < DFF) {
              u32x2 v;
              v[0] = pack2(fmaxf(v0, 0.f), fmaxf(v1, 0.f));
              v[1] = pack2(fmaxf(v2, 0.f), fmaxf(v3, 0.f));
              *reinterpret_cast<u32x2*>(reinterpret_cast<u16*>(ws + WS_H) + (size_t)tok * DFF + nf) = v;
            }
          }
        }
      }
    }
  }
}

__global__ __launch_bounds__(256, 2) void k1_proj(const u16* __restrict__ x, const u16* __restrict__ wqkv,
                                               const u16* __restrict__ w1, const u16* __restrict__ gate_w,
                                               unsigned char* __restrict__ ws) {
  __shared__ __attribute__((aligned(16))) u16 sA[2][K1_BM][K1_LDA];
  __shared__ __attribute__((aligned(16))) u16 sB[2][K1_BK][K1_LDB];
  const int tid = threadIdx.x;
  const int lane = tid & 63;
  const int wave = tid >> 6;
  const int bid = blockIdx.x;
  if (bid >= K1_NT * K1_MT) {
    const int r0 = (bid - K1_NT * K1_MT) * 32 + wave * 8;
    float* g = reinterpret_cast<float*>(ws + WS_G);
    bf16x8 gw = *reinterpret_cast<const bf16x8*>(gate_w + lane * 8);
    bf16x8 xv[8];
#pragma unroll
    for (int i = 0; i < 8; ++i) xv[i] = *reinterpret_cast<const bf16x8*>(x + (size_t)(r0 + i) * DM + lane * 8);
#pragma unroll
    for (int i = 0; i < 8; ++i) {
      float s = 0.f;
#pragma unroll
      for (int j = 0; j < 8; ++j) s += bf2f((u16)xv[i][j]) * bf2f((u16)gw[j]);
#pragma unroll
      for (int o = 32; o > 0; o >>= 1) s += __shfl_xor(s, o);
      if (lane == 0) g[r0 + i] = 1.f / (1.f + __expf(-s));
    }
    return;
  }

  const int nt = bid % K1_NT;
  const int mt = bid / K1_NT;
  if (nt >= 8 && nt < 12) k1_body<true>(x, wqkv, w1, ws, sA, sB, nt, mt);
  else k1_body<false>(x, wqkv, w1, ws, sA, sB, nt, mt);
}

#define G_BK 64
#define G_LDA 72
#define G_LDB 96
#define G_SMEM_ELEMS (2 * (64 * G_LDA + G_BK * G_LDB))

__device__ __forceinline__ f32x16 gemm64(const u16* __restrict__ A, int lda, const u16* __restrict__ B, int ldb,
                                         int K, u16* smem) {
  u16* sA = smem;
  u16* sB = smem + 2 * 64 * G_LDA;
  const int tid = threadIdx.x, lane = tid & 63, wave = tid >> 6;
  const int wm = wave >> 1, wn = wave & 1;
  const int h = lane >> 5, l31 = lane & 31;
  const int blk16 = (lane >> 4) & 1, q4 = (lane & 15) >> 2, p4 = lane & 3;
  const int a_row = tid >> 3, a_col = (tid & 7) * 8;
  const int b_row = tid >> 3, b_col = (tid & 7) * 8;
  f32x16 acc;
#pragma unroll
  for (int r = 0; r < 16; ++r) acc[r] = 0.f;
  const int NKT = (K + G_BK - 1) / G_BK;
  u32x4 ra[2][2], rb[2][2];
  auto gload = [&](int kt, int st) {
    const int k0 = kt * G_BK;
#pragma unroll
    for (int i = 0; i < 2; ++i) {
      ra[st][i] = (k0 + a_col < K) ? *reinterpret_cast<const u32x4*>(A + (size_t)(a_row + 32 * i) * lda + k0 + a_col) : (u32x4){0u, 0u, 0u, 0u};
      rb[st][i] = (k0 + b_row + 32 * i < K) ? *reinterpret_cast<const u32x4*>(B + (size_t)(k0 + b_row + 32 * i) * ldb + b_col) : (u32x4){0u, 0u, 0u, 0u};
    }
  };
  auto swrite = [&](int buf, int st) {
#pragma unroll
    for (int i = 0; i < 2; ++i) {
      *reinterpret_cast<u32x4*>(&sA[(buf * 64 + a_row + 32 * i) * G_LDA + a_col]) = ra[st][i];
      *reinterpret_cast<u32x4*>(&sB[(buf * G_BK + b_row + 32 * i) * G_LDB + b_col]) = rb[st][i];
    }
  };
  auto compute = [&](int buf) {
#pragma unroll
    for (int s = 0; s < 4; ++s) {
      bf16x8 af = *reinterpret_cast<const bf16x8*>(&sA[(buf * 64 + wm * 32 + l31) * G_LDA + s * 16 + 8 * h]);
      bf16x4 t0 = tr_read(&sB[(buf * G_BK + s * 16 + 8 * h + q4) * G_LDB + wn * 32 + 16 * blk16 + 4 * p4]);
      bf16x4 t1 = tr_read(&sB[(buf * G_BK + s * 16 + 8 * h + 4 + q4) * G_LDB + wn * 32 + 16 * blk16 + 4 * p4]);
      bf16x8 bfr = (bf16x8){t0[0], t0[1], t0[2], t0[3], t1[0], t1[1], t1[2], t1[3]};
      acc = mfma32(bfr, af, acc);
    }
  };
  gload(0, 0);
  gload(1, 1);
  swrite(0, 0);
  __syncthreads();
  for (int kt = 0; kt < NKT; kt += 2) {
    if (kt + 2 < NKT) gload(kt + 2, 0);
    compute(0);
    if (kt + 1 < NKT) {
      swrite(1, 1);
      __syncthreads();
      if (kt + 3 < NKT) gload(kt + 3, 1);
      compute(1);
      if (kt + 2 < NKT) {
        swrite(0, 0);
        __syncthreads();
      }
    }
  }
  return acc;
}

#define AT_KC 128

__global__ __launch_bounds__(256, 2) void k2_attn(unsigned char* __restrict__ ws, const u16* __restrict__ w2) {
  __shared__ __attribute__((aligned(16))) u16 smem[G_SMEM_ELEMS];
  const int tid = threadIdx.x, lane = tid & 63, wave = tid >> 6;
  if (blockIdx.x < 256) {
    const int nt = blockIdx.x & 7, mt = blockIdx.x >> 3;
    const u16* hg = reinterpret_cast<const u16*>(ws + WS_H);
    f32x16 acc = gemm64(hg + (size_t)mt * 64 * DFF, DFF, w2 + nt * 64, DM, DFF, smem);
    float* bo = reinterpret_cast<float*>(ws + WS_B) + (size_t)(mt * 64 + (wave >> 1) * 32 + (lane & 31)) * DM + nt * 64 + (wave & 1) * 32 + 4 * (lane >> 5);
#pragma unroll
    for (int gq = 0; gq < 4; ++gq)
      *reinterpret_cast<f32x4*>(bo + 8 * gq) = (f32x4){acc[4 * gq], acc[4 * gq + 1], acc[4 * gq + 2], acc[4 * gq + 3]};
    return;
  }
  const int ab = blockIdx.x - 256;
  const int head = ab >> 4;
  const int qt = ab & 15;
  const int h = lane >> 5, l31 = lane & 31;
  const u16* qg = reinterpret_cast<const u16*>(ws + WS_Q) + (size_t)head * SEQ * 16;
  const u16* kg = reinterpret_cast<const u16*>(ws + WS_K) + (size_t)head * SEQ * 16;
  const u16* vg = reinterpret_cast<const u16*>(ws + WS_V) + (size_t)head * SEQ * 16;

  const int q0 = qt * 128 + wave * 32;
  const bf16x8 qf = *reinterpret_cast<const bf16x8*>(qg + (size_t)(q0 + l31) * 16 + 8 * h);

  const int CH = AT_KC * 16;
  *reinterpret_cast<u32x4*>(&smem[4 * CH + tid * 8]) = (u32x4){0x3F803F80u, 0x3F803F80u, 0x3F803F80u, 0x3F803F80u};

  f32x16 oacc;
#pragma unroll
  for (int r = 0; r < 16; ++r) oacc[r] = 0.f;
  float m = -INFINITY;

  u32x4 rk, rv;
  rk = *reinterpret_cast<const u32x4*>(kg + tid * 8);
  rv = *reinterpret_cast<const u32x4*>(vg + tid * 8);
  *reinterpret_cast<u32x4*>(&smem[tid * 8]) = rk;
  *reinterpret_cast<u32x4*>(&smem[CH + tid * 8]) = rv;
  __syncthreads();

  const int koff = l31 * 16 + 8 * h;
  const int voff0 = (l31 == 16) ? (4 * CH) : (CH + (l31 & 15) * 16 + 8 * h);
  const int voff1 = (l31 == 16) ? (4 * CH) : (3 * CH + (l31 & 15) * 16 + 8 * h);

  const int NC = SEQ / AT_KC;
  f32x16 z;
#pragma unroll
  for (int r = 0; r < 16; ++r) z[r] = 0.f;

  for (int c = 0; c < NC; ++c) {
    const int buf = c & 1;
    if (c + 1 < NC) {
      rk = *reinterpret_cast<const u32x4*>(kg + (size_t)(c + 1) * CH + tid * 8);
      rv = *reinterpret_cast<const u32x4*>(vg + (size_t)(c + 1) * CH + tid * 8);
    }
    const u16* kb_ = smem + buf * 2 * CH + koff;
    const u16* vb_ = smem + (buf ? voff1 : voff0);
#pragma unroll
    for (int t = 0; t < AT_KC / 64; ++t) {
      bf16x8 kf0 = *reinterpret_cast<const bf16x8*>(kb_ + (t * 64) * 16);
      bf16x8 kf1 = *reinterpret_cast<const bf16x8*>(kb_ + (t * 64 + 32) * 16);
      f32x16 s0 = mfma32(kf0, qf, z);
      f32x16 s1 = mfma32(kf1, qf, z);
      float tm0 = __builtin_fmaxf(__builtin_fmaxf(s0[0], s0[1]), s1[0]);
#pragma unroll
      for (int r = 2; r < 16; r += 2) tm0 = __builtin_fmaxf(__builtin_fmaxf(tm0, s0[r]), s0[r + 1]);
      float tm1 = s1[1];
#pragma unroll
      for (int r = 2; r < 16; r += 2) tm1 = __builtin_fmaxf(__builtin_fmaxf(tm1, s1[r]), s1[r + 1]);
      float tm = __builtin_fmaxf(tm0, tm1);
      {
        auto rr = __builtin_amdgcn_permlane32_swap(__float_as_uint(tm), __float_as_uint(tm), false, false);
        tm = __builtin_fmaxf(__uint_as_float(rr[0]), __uint_as_float(rr[1]));
      }
      const float mn = __builtin_fmaxf(m, tm);
      const float alpha = __builtin_amdgcn_exp2f(m - mn);
      m = mn;
#pragma unroll
      for (int r = 0; r < 9; ++r) oacc[r] *= alpha;
      bf16x8 p[4];
#pragma unroll
      for (int ss = 0; ss < 2; ++ss) {
        u32x4 w0, w1;
#pragma unroll
        for (int jj = 0; jj < 4; ++jj) {
          w0[jj] = pack2(__builtin_amdgcn_exp2f(s0[8 * ss + 2 * jj] - mn), __builtin_amdgcn_exp2f(s0[8 * ss + 2 * jj + 1] - mn));
          w1[jj] = pack2(__builtin_amdgcn_exp2f(s1[8 * ss + 2 * jj] - mn), __builtin_amdgcn_exp2f(s1[8 * ss + 2 * jj + 1] - mn));
        }
        p[ss] = __builtin_bit_cast(bf16x8, w0);
        p[2 + ss] = __builtin_bit_cast(bf16x8, w1);
      }
#pragma unroll
      for (int kb = 0; kb < 4; ++kb) {
        bf16x8 vf = *reinterpret_cast<const bf16x8*>(vb_ + ((t * 4 + kb) * 16) * 16);
        oacc = mfma32(vf, p[kb], oacc);
      }
    }
    if (c + 1 < NC) {
      *reinterpret_cast<u32x4*>(&smem[(buf ^ 1) * 2 * CH + tid * 8]) = rk;
      *reinterpret_cast<u32x4*>(&smem[(buf ^ 1) * 2 * CH + CH + tid * 8]) = rv;
      __syncthreads();
    }
  }
  float l;
  {
    auto rr = __builtin_amdgcn_permlane32_swap(__float_as_uint(oacc[8]), __float_as_uint(oacc[8]), false, false);
    l = __uint_as_float(rr[0]);
  }
  const float inv = 1.f / l;
  u16* og = reinterpret_cast<u16*>(ws + WS_O) + (size_t)(q0 + l31) * DM + head * 16;
#pragma unroll
  for (int g = 0; g < 2; ++g) {
    u32x2 v;
    v[0] = pack2(oacc[4 * g] * inv, oacc[4 * g + 1] * inv);
    v[1] = pack2(oacc[4 * g + 2] * inv, oacc[4 * g + 3] * inv);
    *reinterpret_cast<u32x2*>(og + 8 * g + 4 * h) = v;
  }
}

__global__ __launch_bounds__(256, 2) void k3_out(const unsigned char* __restrict__ ws, const u16* __restrict__ wo,
                                                 u16* __restrict__ out) {
  __shared__ __attribute__((aligned(16))) u16 smem[G_SMEM_ELEMS];
  const int tid = threadIdx.x, lane = tid & 63, wave = tid >> 6;
  const int nt = blockIdx.x & 7, mt = blockIdx.x >> 3;
  const u16* og = reinterpret_cast<const u16*>(ws + WS_O);
  const int tok = mt * 64 + (wave >> 1) * 32 + (lane & 31);
  const int nb = nt * 64 + (wave & 1) * 32 + 4 * (lane >> 5);
  const float* bp = reinterpret_cast<const float*>(ws + WS_B) + (size_t)tok * DM + nb;
  f32x4 bv[4];
#pragma unroll
  for (int gq = 0; gq < 4; ++gq) bv[gq] = *reinterpret_cast<const f32x4*>(bp + 8 * gq);
  const float g = reinterpret_cast<const float*>(ws + WS_G)[tok];
  f32x16 acc = gemm64(og + (size_t)mt * 64 * DM, DM, wo + nt * 64, DM, DM, smem);
  const float g1 = 1.f - g;
#pragma unroll
  for (int gq = 0; gq < 4; ++gq) {
    u32x2 v;
    v[0] = pack2(g * acc[4 * gq] + g1 * bv[gq][0], g * acc[4 * gq + 1] + g1 * bv[gq][1]);
    v[1] = pack2(g * acc[4 * gq + 2] + g1 * bv[gq][2], g * acc[4 * gq + 3] + g1 * bv[gq][3]);
    *reinterpret_cast<u32x2*>(out + (size_t)tok * DM + nb + 8 * gq) = v;
  }
}

extern "C" void kernel_launch(void* const* d_in, const int* in_sizes, int n_in,
                              void* d_out, int out_size, void* d_ws, size_t ws_size,
                              hipStream_t stream) {
  const u16* x = (const u16*)d_in[0];
  const u16* wqkv = (const u16*)d_in[1];
  const u16* wo = (const u16*)d_in[2];
  const u16* w1 = (const u16*)d_in[4];
  const u16* w2 = (const u16*)d_in[5];
  const u16* gw = (const u16*)d_in[6];
  unsigned char* ws = (unsigned char*)d_ws;
  k1_proj<<<K1_NT * K1_MT + 64, 256, 0, stream>>>(x, wqkv, w1, gw, ws);
  k2_attn<<<256 + NH * 16, 256, 0, stream>>>(ws, w2);
  k3_out<<<256, 256, 0, stream>>>(ws, wo, (u16*)d_out);
}
```
